```python
import torch

def _softmax(x, axis=-1):
    return torch.softmax(x, dim=axis)
def _gelu(x):
    return torch.nn.functional.gelu(x, approximate="tanh")
def _silu(x):
    return torch.nn.functional.silu(x)
def _sigmoid(x):
    return torch.sigmoid(x)
def _layernorm(x, axis=-1, eps=1e-5):
    m = x.mean(dim=axis, keepdim=True)
    v = x.var(dim=axis, keepdim=True, unbiased=False)
    return (x - m) / torch.sqrt(v + eps)
def _scatter_add(tgt, idx, src, axis=0):
    return tgt.clone().index_add_(axis, idx, src)
def _segment_sum(src, seg, n):
    out = torch.zeros((n,) + src.shape[1:], dtype=src.dtype)
    return out.index_add_(0, seg, src)
def _pad(x, pw):
    flat = tuple(v for pair in reversed(pw) for v in pair)
    return torch.nn.functional.pad(x, flat)

kernel_name = 'concat_heads__mha_block_cached__gqa_block_cached__decode'

def setup_inputs():

    _w = (lambda a: (lambda t: t.to(torch.bfloat16) if t.is_floating_point() else t)(torch.as_tensor(a)))
    def _A_setup():
        B, Sq, D = 1, 1, 512
        H, Sk, Dh = 32, 8192, 128
        return {
            "x": _w((lambda *s: torch.randn(*s, dtype=torch.bfloat16) * 0.8)(B, Sq, D)),
            "k_cache": _w((lambda *s: torch.randn(*s, dtype=torch.bfloat16) * 0.8)(B, H, Sk, Dh)),
            "v_cache": _w((lambda *s: torch.randn(*s, dtype=torch.bfloat16) * 0.05)(B, H, Sk, Dh)),
            "wq": _w((lambda *s: torch.randn(*s, dtype=torch.bfloat16) * 0.8)(D, H * Dh) * (D ** -0.5)),
            "wo": _w((lambda *s: torch.randn(*s, dtype=torch.bfloat16) * 0.05)(H * Dh, D) * ((H * Dh) ** -0.5)),
        }
    _A_in = _A_setup()

    def _B_setup():
        B, Sq, D = 1, 1, 512
        Hq, Hkv, Sk, Dh = 32, 32, 8192, 128
        assert Hq % Hkv == 0, (Hq, Hkv)
        return {
            "x": _w((lambda *s: torch.randn(*s, dtype=torch.bfloat16) * 0.8)(B, Sq, D)),
            "k_cache": _w((lambda *s: torch.randn(*s, dtype=torch.bfloat16) * 0.8)(B, Hkv, Sk, Dh)),
            "v_cache": _w((lambda *s: torch.randn(*s, dtype=torch.bfloat16) * 0.05)(B, Hkv, Sk, Dh)),
            "wq": _w((lambda *s: torch.randn(*s, dtype=torch.bfloat16) * 0.8)(D, Hq * Dh) * (D ** -0.5)),
            "wo": _w((lambda *s: torch.randn(*s, dtype=torch.bfloat16) * 0.05)(Hq * Dh, D) * ((Hq * Dh) ** -0.5)),
        }
    _B_in = _B_setup()

    return {
        "A_x": _A_in["x"],
        "A_k_cache": _A_in["k_cache"],
        "A_v_cache": _A_in["v_cache"],
        "A_wq": _A_in["wq"],
        "A_wo": _A_in["wo"],
        "B_x": _B_in["x"],
        "B_k_cache": _B_in["k_cache"],
        "B_v_cache": _B_in["v_cache"],
        "B_wq": _B_in["wq"],
        "B_wo": _B_in["wo"],
    }

def reference(A_x, A_k_cache, A_v_cache, A_wq, A_wo, B_x, B_k_cache, B_v_cache, B_wq, B_wo):
    def _A_ref(x, k_cache, v_cache, wq, wo):
        B, Sq, D = x.shape
        H, Dh = 32, 128
        q = (lambda x, *d: x.permute(*d))((x @ wq).reshape(B, Sq, H, Dh), 0, 2, 1, 3)
        scores = torch.einsum("bhqd,bhkd->bhqk", q, k_cache) * (Dh ** -0.5)
        p = _softmax(scores, axis=-1)
        o = torch.einsum("bhqk,bhkd->bhqd", p, v_cache)
        return (lambda x, *d: x.permute(*d))(o, 0, 2, 1, 3).reshape(B, Sq, H * Dh) @ wo

    def _B_ref(x, k_cache, v_cache, wq, wo):
        B, Sq, D = x.shape
        Hq, Dh = 32, 128
        Hkv = k_cache.shape[1]
        rep = Hq // Hkv
        q = (lambda x, *d: x.permute(*d))((x @ wq).reshape(B, Sq, Hq, Dh), 0, 2, 1, 3)
        k = (lambda x, n, axis: torch.repeat_interleave(x, n, dim=axis))(k_cache, rep, axis=1)
        v = (lambda x, n, axis: torch.repeat_interleave(x, n, dim=axis))(v_cache, rep, axis=1)
        scores = torch.einsum("bhqd,bhkd->bhqk", q, k) * (Dh ** -0.5)
        p = _softmax(scores, axis=-1)
        o = torch.einsum("bhqk,bhkd->bhqd", p, v)
        return (lambda x, *d: x.permute(*d))(o, 0, 2, 1, 3).reshape(B, Sq, Hq * Dh) @ wo

    a = _A_ref(A_x, A_k_cache, A_v_cache, A_wq, A_wo)
    b = _B_ref(B_x, B_k_cache, B_v_cache, B_wq, B_wo)
    return torch.concatenate([a, b], axis=-1)
```

```cpp
#include <hip/hip_runtime.h>
#include <stdint.h>

#define NW 8
#define TL 4
#define DEPTH 4
#define PH1 3
#define WO_PER_TILE 2
#define W_NT 0
#define ST_AUX 16
#define LD_AUX 16
#define UNPACK_AT (NT - 3)
#define WO_START 0
#define PIN 1
#define POLL_SLEEP 1

typedef unsigned int u32;
typedef unsigned short u16;
typedef unsigned long long u64;
using u32x4 = __attribute__((ext_vector_type(4))) unsigned;
using u32x2 = __attribute__((ext_vector_type(2))) unsigned;
using f32x4 = __attribute__((ext_vector_type(4))) float;
using f32x2 = __attribute__((ext_vector_type(2))) float;
using bf16x2 = __attribute__((ext_vector_type(2))) __bf16;
typedef __attribute__((address_space(1))) u64 gu64;

constexpr int DM = 512, NH = 32, SK = 8192, DH = 128;
constexpr int NSPLIT = 4;
constexpr int KEYS_WG = SK / NSPLIT;
constexpr int KEYS_WAVE = KEYS_WG / NW;
constexpr int NT = KEYS_WAVE / (4 * TL);
constexpr int RPL = DM / NW / 4;
constexpr int CPW = DM / NW;
constexpr int LPR = CPW / 8;
constexpr int NWO = DH / (64 / LPR);
constexpr float C2 = 0.08838834764831845f * 1.4426950408889634f;
static_assert(NWO <= WO_PER_TILE * NT, "wo loads must fit beside the tiles");
static_assert(NW == 8, "this version: 8 waves");

struct Args {
  const u16* x[2];
  const u16* k[2];
  const u16* v[2];
  const u16* wq[2];
  const u16* wo[2];
  u16* out;
  char* ws;
};

__device__ __forceinline__ float dot2(u32 a, u32 b, float c) {
  return __builtin_amdgcn_fdot2_f32_bf16(__builtin_bit_cast(bf16x2, a), __builtin_bit_cast(bf16x2, b), c, false);
}
template <int CTRL> __device__ __forceinline__ float dpp(float x) {
  return __builtin_bit_cast(float, __builtin_amdgcn_mov_dpp(__builtin_bit_cast(int, x), CTRL, 0xf, 0xf, true));
}
__device__ __forceinline__ float row16_sum(float x) {
  x += dpp<0xB1>(x);
  x += dpp<0x4E>(x);
  x += dpp<0x141>(x);
  x += dpp<0x140>(x);
  return x;
}
__device__ __forceinline__ float row16_max(float x) {
  x = fmaxf(x, dpp<0xB1>(x));
  x = fmaxf(x, dpp<0x4E>(x));
  x = fmaxf(x, dpp<0x141>(x));
  x = fmaxf(x, dpp<0x140>(x));
  return x;
}
__device__ __forceinline__ float lo_f(u32 v) { return __builtin_bit_cast(float, v << 16); }
__device__ __forceinline__ float hi_f(u32 v) { return __builtin_bit_cast(float, v & 0xffff0000u); }
__device__ __forceinline__ u32 f2u(float v) { return __builtin_bit_cast(u32, v); }
__device__ __forceinline__ float u2f(u32 v) { return __builtin_bit_cast(float, v); }
__device__ __forceinline__ float tr32(float a, float b) {
  auto s = __builtin_amdgcn_permlane32_swap(f2u(a), f2u(b), false, false);
  return u2f(s[0]) + u2f(s[1]);
}
__device__ __forceinline__ float tr16(float a, float b) {
  auto s = __builtin_amdgcn_permlane16_swap(f2u(a), f2u(b), false, false);
  return u2f(s[0]) + u2f(s[1]);
}
__device__ __forceinline__ float xrow_max(float x) {
  auto s = __builtin_amdgcn_permlane16_swap(f2u(x), f2u(x), false, false);
  x = fmaxf(u2f(s[0]), u2f(s[1]));
  auto t = __builtin_amdgcn_permlane32_swap(f2u(x), f2u(x), false, false);
  return fmaxf(u2f(t[0]), u2f(t[1]));
}
__device__ __forceinline__ float xrow_sum(float x) {
  auto s = __builtin_amdgcn_permlane16_swap(f2u(x), f2u(x), false, false);
  x = u2f(s[0]) + u2f(s[1]);
  auto t = __builtin_amdgcn_permlane32_swap(f2u(x), f2u(x), false, false);
  return u2f(t[0]) + u2f(t[1]);
}
__device__ __forceinline__ u32 pack_bf16(float lo, float hi) {
  bf16x2 r;
  r[0] = (__bf16)lo;
  r[1] = (__bf16)hi;
  return __builtin_bit_cast(u32, r);
}
typedef __amdgpu_buffer_rsrc_t rsrc_t;
__device__ __forceinline__ rsrc_t make_rsrc(const void* p, u32 bytes) {
  return __builtin_amdgcn_make_buffer_rsrc(const_cast<void*>(p), 0, bytes, 0x00020000);
}
__device__ __forceinline__ u32x4 ld_kv(rsrc_t rs, u32 voff, u32 soff) {
  return __builtin_amdgcn_raw_buffer_load_b128(rs, voff, soff, 2);
}
__device__ __forceinline__ u32x4 ld_w(rsrc_t rs, u32 voff, u32 soff) {
  return __builtin_amdgcn_raw_buffer_load_b128(rs, voff, soff, W_NT ? 2 : 0);
}

constexpr int LDS_QP = 0;
constexpr int LDS_AP = LDS_QP + NW * DH;
constexpr int LDS_ML = LDS_AP + NW * DH;
constexpr int LDS_A = LDS_ML + 2 * NW;
constexpr int LDS_RED = LDS_A + NW * DH;
constexpr int LDS_EP = LDS_RED + 512;
constexpr int LDS_FLOATS = LDS_EP + 4;

constexpr size_t WS_Y = 0;
constexpr size_t WS_LAM = 1u << 20;
constexpr size_t WS_EPOCH = WS_LAM + (256u << 10);

__global__ void __launch_bounds__(NW * 64, NW / 4) attn_kernel(Args a) {
  __shared__ __attribute__((aligned(16))) float lds[LDS_FLOATS];
  const int bid = blockIdx.x;
  const int hg = bid & 63, s = bid >> 6;
  const int blk = hg >> 5, h = hg & 31;
  const int tid = threadIdx.x, lane = tid & 63;
  const int w = __builtin_amdgcn_readfirstlane(tid >> 6);
  const int r = lane >> 4, c = lane & 15;

  char* wsp = a.ws;
  u16* outp = a.out;
  asm volatile("" : "+s"(wsp), "+s"(outp));
  const rsrc_t xs = make_rsrc(a.x[blk], DM * 2);
  const rsrc_t wqs = make_rsrc(a.wq[blk], DM * NH * DH * 2);
  const rsrc_t wos = make_rsrc(a.wo[blk] + (size_t)h * DH * DM, DH * DM * 2);
  const rsrc_t ks = make_rsrc(a.k[blk] + ((size_t)h * SK + (size_t)s * KEYS_WG) * DH, KEYS_WG * DH * 2);
  const rsrc_t vs = make_rsrc(a.v[blk] + ((size_t)h * SK + (size_t)s * KEYS_WG) * DH, KEYS_WG * DH * 2);

#define TILE_OFF(i, t) ((((i) * NW + w) * (4 * TL) + 16 * ((t) >> 2)) * (DH * 2))
#define LDK(i, t) ld_kv(ks, loff + ((t) & 3) * 1024, TILE_OFF(i, t))
#define LDV(i, t) ld_kv(vs, loff + ((t) & 3) * 1024, TILE_OFF(i, t))
  const u32 loff = lane * 16;
  u32x4 Kr[DEPTH][TL], Vr[DEPTH][TL];

  u64 epoch = 0;
  if (tid == 0) epoch = __hip_atomic_load((gu64*)(wsp + WS_EPOCH + 256 * blk), __ATOMIC_RELAXED, __HIP_MEMORY_SCOPE_AGENT);
  u32x4 xr[RPL / 8];
#pragma unroll
  for (int j = 0; j < RPL / 8; ++j) xr[j] = __builtin_amdgcn_raw_buffer_load_b128(xs, r * RPL * 2 + 16 * j, w * (4 * RPL * 2), 0);
  u32x4 wr[RPL];
  constexpr int NKV1 = PH1 * 2 * TL;
  constexpr int NI = RPL > NKV1 ? RPL : NKV1;
#pragma unroll
  for (int j = 0; j < NI; ++j) {
    if (j < RPL) wr[j] = ld_w(wqs, r * (RPL * NH * DH * 2) + 16 * c, (w * 4 * RPL + j) * (NH * DH * 2) + h * (DH * 2));
    if (j < NKV1) {
      const int d = j / (2 * TL), t = j % TL;
      if ((j / TL) & 1) Vr[d][t] = LDV(d, t); else Kr[d][t] = LDK(d, t);
    }
    __builtin_amdgcn_sched_barrier(0);
  }
  __builtin_amdgcn_s_barrier();
  __builtin_amdgcn_sched_barrier(0);
#pragma unroll
  for (int d = PH1; d < DEPTH; ++d) {
#pragma unroll
    for (int t = 0; t < TL; ++t) Kr[d][t] = LDK(d, t);
#pragma unroll
    for (int t = 0; t < TL; ++t) Vr[d][t] = LDV(d, t);
  }
  __builtin_amdgcn_sched_barrier(0);

  float qa[8];
#pragma unroll
  for (int k = 0; k < 8; ++k) qa[k] = 0.f;
#pragma unroll
  for (int j = 0; j < RPL; ++j) {
    const u32 xw = xr[j >> 3][(j >> 1) & 3];
    const float xv = (j & 1) ? hi_f(xw) : lo_f(xw);
#pragma unroll
    for (int k = 0; k < 4; ++k) {
      qa[2 * k] = fmaf(xv, lo_f(wr[j][k]), qa[2 * k]);
      qa[2 * k + 1] = fmaf(xv, hi_f(wr[j][k]), qa[2 * k + 1]);
    }
  }
  {
    float y0 = tr32(qa[0], qa[4]), y1 = tr32(qa[1], qa[5]), y2 = tr32(qa[2], qa[6]), y3 = tr32(qa[3], qa[7]);
    float z0 = tr16(y0, y2), z1 = tr16(y1, y3);
    *reinterpret_cast<f32x2*>(&lds[LDS_QP + w * DH + 8 * c + 2 * r]) = f32x2{z0, z1};
  }
  if (tid == 0) { *reinterpret_cast<u64*>(&lds[LDS_EP]) = epoch; *reinterpret_cast<u32*>(&lds[LDS_EP + 2]) = 0u; }
  __syncthreads();
  u32 q4[4];
  {
    float s0 = 0.f, s1 = 0.f;
#pragma unroll
    for (int i = 0; i < NW; ++i) {
      const f32x2 p = *reinterpret_cast<const f32x2*>(&lds[LDS_QP + i * DH + 8 * c + 2 * r]);
      s0 += p[0];
      s1 += p[1];
    }
    const u32 qd = pack_bf16(s0, s1);
    auto ab = __builtin_amdgcn_permlane16_swap(qd, qd, false, false);
    auto q02 = __builtin_amdgcn_permlane32_swap(ab[0], ab[0], false, false);
    auto q13 = __builtin_amdgcn_permlane32_swap(ab[1], ab[1], false, false);
    q4[0] = q02[0]; q4[2] = q02[1]; q4[1] = q13[0]; q4[3] = q13[1];
  }

  const int rr = lane / LPR, cc = lane % LPR;
  u32x4 wor[NWO];
  float wof[NWO][8];
  float m = 0.f, l = 0.f, acc[8];
#pragma unroll
  for (int i = 0; i < NT; ++i) {
    const int d = i % DEPTH;
    float sc[TL];
#pragma unroll
    for (int t = 0; t < TL; ++t) {
      float x = dot2(Kr[d][t][0], q4[0], 0.f);
      x = dot2(Kr[d][t][1], q4[1], x);
      x = dot2(Kr[d][t][2], q4[2], x);
      x = dot2(Kr[d][t][3], q4[3], x);
      sc[t] = x;
      if (i + DEPTH < NT) Kr[d][t] = LDK(i + DEPTH, t);
    }
#pragma unroll
    for (int jj = 0; jj < WO_PER_TILE; ++jj) {
      const int j = (i - WO_START) * WO_PER_TILE + jj;
      if (j >= 0 && j < NWO) wor[j] = ld_w(wos, rr * (NWO * DM * 2) + 16 * cc + (j & 3) * (DM * 2), w * (CPW * 2) + (j >> 2) * (4 * DM * 2));
    }
    __builtin_amdgcn_sched_barrier(0);
#pragma unroll
    for (int t = 0; t < TL; ++t) sc[t] = row16_sum(sc[t]) * C2;
    float mt = sc[0];
#pragma unroll
    for (int t = 1; t < TL; ++t) mt = fmaxf(mt, sc[t]);
    float p[TL];
    if (i == 0) {
      m = mt;
#pragma unroll
      for (int t = 0; t < TL; ++t) p[t] = __builtin_amdgcn_exp2f(sc[t] - m);
      l = p[0];
#pragma unroll
      for (int t = 1; t < TL; ++t) l += p[t];
#pragma unroll
      for (int k = 0; k < 8; ++k) acc[k] = 0.f;
    } else {
      const float mn = fmaxf(m, mt);
      const float alpha = __builtin_amdgcn_exp2f(m - mn);
      m = mn;
#pragma unroll
      for (int t = 0; t < TL; ++t) p[t] = __builtin_amdgcn_exp2f(sc[t] - m);
      l *= alpha;
#pragma unroll
      for (int t = 0; t < TL; ++t) l += p[t];
#pragma unroll
      for (int k = 0; k < 8; ++k) acc[k] *= alpha;
    }
#pragma unroll
    for (int t = 0; t < TL; ++t) {
#pragma unroll
      for (int k = 0; k < 4; ++k) {
        acc[2 * k] = fmaf(p[t], lo_f(Vr[d][t][k]), acc[2 * k]);
        acc[2 * k + 1] = fmaf(p[t], hi_f(Vr[d][t][k]), acc[2 * k + 1]);
      }
      if (i + DEPTH < NT) Vr[d][t] = LDV(i + DEPTH, t);
      __builtin_amdgcn_sched_barrier(0);
    }
    if (i == UNPACK_AT) {
#pragma unroll
      for (int j = 0; j < NWO; ++j)
#pragma unroll
        for (int k = 0; k < 4; ++k) {
          wof[j][2 * k] = lo_f(wor[j][k]);
          wof[j][2 * k + 1] = hi_f(wor[j][k]);
          asm volatile("" : "+v"(wof[j][2 * k]), "+v"(wof[j][2 * k + 1]));
        }
      __builtin_amdgcn_sched_barrier(0);
    }
  }

  {
    const float M = xrow_max(m);
    const float f = __builtin_amdgcn_exp2f(m - M);
    const float L = xrow_sum(l * f);
#pragma unroll
    for (int k = 0; k < 8; ++k) acc[k] *= f;
    float y0 = tr32(acc[0], acc[4]), y1 = tr32(acc[1], acc[5]), y2 = tr32(acc[2], acc[6]), y3 = tr32(acc[3], acc[7]);
    float z0 = tr16(y0, y2), z1 = tr16(y1, y3);
    *reinterpret_cast<f32x2*>(&lds[LDS_AP + w * DH + 8 * c + 2 * r]) = f32x2{z0, z1};
    if (lane == 0) *reinterpret_cast<f32x2*>(&lds[LDS_ML + 2 * w]) = f32x2{M, L};
  }
  const int src = h * NSPLIT + s;
  const u64 tag = (*reinterpret_cast<const u64*>(&lds[LDS_EP]) + 1);
  const rsrc_t ib = make_rsrc(wsp + WS_Y + (size_t)blk * (32 * 128 * 128), 32 * 128 * 128);
  __syncthreads();
  float lam;
  {
    const f32x2 ml = *reinterpret_cast<const f32x2*>(&lds[LDS_ML + 2 * (c % NW)]);
    const float M = row16_max(ml[0]);
    const float f = __builtin_amdgcn_exp2f(ml[0] - M);
    const float L = row16_sum(f * ml[1]) * 0.5f;
    const float finv = f * __builtin_amdgcn_rcpf(L);
    lam = M + __builtin_amdgcn_logf(L);
    float a0 = 0.f, a1 = 0.f;
#pragma unroll
    for (int i = 0; i < NW; ++i) {
      const f32x2 p = *reinterpret_cast<const f32x2*>(&lds[LDS_AP + i * DH + 2 * lane]);
      const float wi = u2f(__builtin_amdgcn_readlane(f2u(finv), i));
      a0 = fmaf(wi, p[0], a0);
      a1 = fmaf(wi, p[1], a1);
    }
    *reinterpret_cast<f32x2*>(&lds[LDS_A + w * DH + 2 * lane]) = f32x2{a0, a1};
  }
  __builtin_amdgcn_fence(__ATOMIC_RELEASE, "wavefront");
  __builtin_amdgcn_wave_barrier();
  __builtin_amdgcn_fence(__ATOMIC_ACQUIRE, "wavefront");
  {
    float y[8];
#pragma unroll
    for (int k = 0; k < 8; ++k) y[k] = 0.f;
#pragma unroll
    for (int j4 = 0; j4 < NWO / 4; ++j4) {
      const f32x4 av = *reinterpret_cast<const f32x4*>(&lds[LDS_A + w * DH + rr * NWO + 4 * j4]);
#pragma unroll
      for (int jj = 0; jj < 4; ++jj)
#pragma unroll
        for (int k = 0; k < 8; ++k) y[k] = fmaf(av[jj], wof[4 * j4 + jj][k], y[k]);
    }
    float y0 = tr32(y[0], y[4]), y1 = tr32(y[1], y[5]), y2 = tr32(y[2], y[6]), y3 = tr32(y[3], y[7]);
    float z0 = tr16(y0, y2), z1 = tr16(y1, y3);
    z0 += dpp<0x128>(z0);
    z1 += dpp<0x128>(z1);
    const u32 lb = f2u(lam) >> (4 * cc);
    const u32 thi = (u32)(tag >> 6), tlo = (u32)tag << 26;
    u32x4 gr;
    gr[0] = tlo | ((lb & 3u) << 24) | ((f2u(z0) + 0x80u) >> 8);
    gr[1] = thi;
    gr[2] = tlo | (((lb >> 2) & 3u) << 24) | ((f2u(z1) + 0x80u) >> 8);
    gr[3] = thi;
    if ((lane & 8) == 0) __builtin_amdgcn_raw_buffer_store_b128(gr, ib, (lane >> 4) * (128 * 128) + cc * 16, (w * 4 * 128 + src) * 128, ST_AUX);
  }
  if (s == 0) {
    const int part = lane & 3;
    const u32 thi = (u32)(tag >> 6), tlo = (u32)tag & 63u;
    u32x4 ga, gb;
    for (u32 spins = 0;; ++spins) {
      ga = __builtin_amdgcn_raw_buffer_load_b128(ib, (tid >> 2) * 128 + part * 16, h * (128 * 128), LD_AUX);
      gb = __builtin_amdgcn_raw_buffer_load_b128(ib, (tid >> 2) * 128 + part * 16 + 64, h * (128 * 128), LD_AUX);
      const bool ok = ga[1] == thi && ga[3] == thi && gb[1] == thi && gb[3] == thi && (ga[0] >> 26) == tlo && (ga[2] >> 26) == tlo &&
                      (gb[0] >> 26) == tlo && (gb[2] >> 26) == tlo;
      if (__all(ok) || spins > (1u << 16)) break;
      __builtin_amdgcn_s_sleep(POLL_SLEEP);
      asm volatile("" ::: "memory");
    }
    u32 lb = (((ga[0] >> 24) & 3u) | ((ga[2] >> 22) & 12u) | ((gb[0] >> 8) & 0x30000u) | ((gb[2] >> 6) & 0xC0000u)) << (4 * part);
    lb |= f2u(dpp<0xB1>(u2f(lb)));
    lb |= f2u(dpp<0x4E>(u2f(lb)));
    const float lm = u2f(lb);
    float M = fmaxf(lm, dpp<0x124>(lm));
    M = fmaxf(M, dpp<0x128>(M));
    const float e = __builtin_amdgcn_exp2f(lm - M);
    float L = e + dpp<0x124>(e);
    L += dpp<0x128>(L);
    const float wt = e * __builtin_amdgcn_rcpf(L);
    const float u0 = tr32(u2f(ga[0] << 8) * wt, u2f(gb[0] << 8) * wt);
    const float u1 = tr32(u2f(ga[2] << 8) * wt, u2f(gb[2] << 8) * wt);
    float v = tr16(u0, u1);
    v += dpp<0x124>(v);
    v += dpp<0x128>(v);
    float* red = &lds[LDS_RED];
    if ((lane & 12) == 0) red[8 * (((lane >> 4) << 2) + part) + w] = v;
    __syncthreads();
    if (w == 0) {
      const f32x4 r0 = *reinterpret_cast<const f32x4*>(&red[8 * c]);
      const f32x4 r1 = *reinterpret_cast<const f32x4*>(&red[8 * c + 4]);
      const float tot = ((r0[0] + r0[1]) + (r0[2] + r0[3])) + ((r1[0] + r1[1]) + (r1[2] + r1[3]));
      const u32 pk = pack_bf16(tot, dpp<0x104>(tot));
      if (tid < 16 && (tid & 4) == 0) {
        const int ccx = (tid & 3) + ((tid >> 3) << 2);
        *reinterpret_cast<u32*>(outp + blk * DM + 64 * (h >> 2) + 8 * ccx + 2 * (h & 3)) = pk;
      }
      if (tid == 0 && h == 0) __hip_atomic_store((gu64*)(wsp + WS_EPOCH + 256 * blk), tag, __ATOMIC_RELAXED, __HIP_MEMORY_SCOPE_AGENT);
    }
  }
}

extern "C" void kernel_launch(void* const* d_in, const int* in_sizes, int n_in, void* d_out, int out_size, void* d_ws,
                              size_t ws_size, hipStream_t stream) {
  Args a;
  a.x[0] = (const u16*)d_in[0];  a.k[0] = (const u16*)d_in[1];  a.v[0] = (const u16*)d_in[2];
  a.wq[0] = (const u16*)d_in[3]; a.wo[0] = (const u16*)d_in[4];
  a.x[1] = (const u16*)d_in[5];  a.k[1] = (const u16*)d_in[6];  a.v[1] = (const u16*)d_in[7];
  a.wq[1] = (const u16*)d_in[8]; a.wo[1] = (const u16*)d_in[9];
  a.out = (u16*)d_out;
  a.ws = (char*)d_ws;
  attn_kernel<<<dim3(64 * NSPLIT), dim3(NW * 64), 0, stream>>>(a);
}
```

```cpp
#include <hip/hip_runtime.h>
#include <stdint.h>

#define DEPTH 4
#define PH1 3
#define WO_PER_TILE 4
#define PERM_AT (NT - 3)
#define POLL_SLEEP 1

typedef unsigned int u32;
typedef unsigned short u16;
typedef unsigned long long u64;
using u32x8 = __attribute__((ext_vector_type(8))) unsigned;
using u32x4 = __attribute__((ext_vector_type(4))) unsigned;
using u32x2 = __attribute__((ext_vector_type(2))) unsigned;
using f32x4 = __attribute__((ext_vector_type(4))) float;
using f32x2 = __attribute__((ext_vector_type(2))) float;
using bf16x2 = __attribute__((ext_vector_type(2))) __bf16;
using bf16x8 = __attribute__((ext_vector_type(8))) __bf16;
typedef __attribute__((address_space(1))) u64 gu64;
typedef __attribute__((address_space(1))) u16 gu16;
typedef __attribute__((address_space(3))) float lds_t;

constexpr int NW = 8;
constexpr int TL = 4;
constexpr int DM = 512, NH = 32, SK = 8192, DH = 128;
constexpr int NSPLIT = 4;
constexpr int KEYS_WG = SK / NSPLIT;
constexpr int NT = KEYS_WG / NW / (4 * TL);
constexpr int RPL = DM / NW / 4;
constexpr int NWO = 32;
constexpr float C2 = 0.08838834764831845f * 1.4426950408889634f;
constexpr int AUX_NT = 2, AUX_SC1 = 16;
static_assert(NWO <= WO_PER_TILE * PERM_AT, "wo loads must fit beside the tiles");

struct Args {
  const u16* x[2];
  const u16* k[2];
  const u16* v[2];
  const u16* wq[2];
  const u16* wo[2];
  u16* out;
  char* ws;
};

__device__ __forceinline__ u32 f2u(float v) { return __builtin_bit_cast(u32, v); }
__device__ __forceinline__ float u2f(u32 v) { return __builtin_bit_cast(float, v); }
__device__ __forceinline__ float dot2(u32 a, u32 b, float c) {
  return __builtin_amdgcn_fdot2_f32_bf16(__builtin_bit_cast(bf16x2, a), __builtin_bit_cast(bf16x2, b), c, false);
}
template <int CTRL> __device__ __forceinline__ float dpp(float x) {
  return u2f((u32)__builtin_amdgcn_mov_dpp((int)f2u(x), CTRL, 0xf, 0xf, true));
}
__device__ __forceinline__ float row16_sum(float x) {
  x += dpp<0xB1>(x);
  x += dpp<0x4E>(x);
  x += dpp<0x141>(x);
  x += dpp<0x140>(x);
  return x;
}
__device__ __forceinline__ float lo_f(u32 v) { return u2f(v << 16); }
__device__ __forceinline__ float hi_f(u32 v) { return u2f(v & 0xffff0000u); }
__device__ __forceinline__ float tr32(float a, float b) {
  auto s = __builtin_amdgcn_permlane32_swap(f2u(a), f2u(b), false, false);
  return u2f(s[0]) + u2f(s[1]);
}
__device__ __forceinline__ float tr16(float a, float b) {
  auto s = __builtin_amdgcn_permlane16_swap(f2u(a), f2u(b), false, false);
  return u2f(s[0]) + u2f(s[1]);
}
__device__ __forceinline__ float xrow_max(float x) {
  auto s = __builtin_amdgcn_permlane16_swap(f2u(x), f2u(x), false, false);
  x = fmaxf(u2f(s[0]), u2f(s[1]));
  auto t = __builtin_amdgcn_permlane32_swap(f2u(x), f2u(x), false, false);
  return fmaxf(u2f(t[0]), u2f(t[1]));
}
__device__ __forceinline__ float xrow_sum(float x) {
  x = tr16(x, x);
  return tr32(x, x);
}
__device__ __forceinline__ u32 pack_bf16(float lo, float hi) {
  bf16x2 r;
  r[0] = (__bf16)lo;
  r[1] = (__bf16)hi;
  return __builtin_bit_cast(u32, r);
}
typedef __amdgpu_buffer_rsrc_t rsrc_t;
__device__ __forceinline__ rsrc_t make_rsrc(const void* p, u32 bytes) {
  return __builtin_amdgcn_make_buffer_rsrc(const_cast<void*>(p), 0, bytes, 0x00020000);
}

constexpr int AROW = 68;
constexpr int LDS_QP = 0;
constexpr int LDS_AP = LDS_QP + NW * DH;
constexpr int LDS_ML = LDS_AP + 16 * AROW;
constexpr int LDS_RED = LDS_ML + 2 * NW;
constexpr int LDS_EP = LDS_RED + 128;
constexpr int LDS_FLOATS = LDS_EP + 4;

constexpr size_t WS_Y = 0;
constexpr size_t WS_EPOCH = 1u << 20;


__global__ void __launch_bounds__(NW * 64, NW / 4) attn_kernel(Args a) {
  __shared__ __attribute__((aligned(16))) float lds[LDS_FLOATS];
  const int bid = blockIdx.x;
  const int hg = bid & 63, s = bid >> 6;
  const int blk = hg >> 5, h = hg & 31;
  const int tid = threadIdx.x, lane = tid & 63;
  const int w = __builtin_amdgcn_readfirstlane(tid >> 6);
  const int r = lane >> 4, c = lane & 15;

  char* wsp = a.ws;
  u16* outp = a.out;
  const u16 *xp = a.x[blk], *wqp = a.wq[blk], *wop = a.wo[blk], *kp = a.k[blk], *vp = a.v[blk];
  asm volatile("" : "+s"(wsp), "+s"(outp), "+s"(xp), "+s"(wqp), "+s"(wop), "+s"(kp), "+s"(vp));
  const rsrc_t xs = make_rsrc(xp, DM * 2);
  const rsrc_t wqs = make_rsrc(wqp, DM * NH * DH * 2);
  const rsrc_t wos = make_rsrc(wop + (size_t)h * DH * DM, DH * DM * 2);
  const rsrc_t ks = make_rsrc(kp + ((size_t)h * SK + (size_t)s * KEYS_WG) * DH, KEYS_WG * DH * 2);
  const rsrc_t vs = make_rsrc(vp + ((size_t)h * SK + (size_t)s * KEYS_WG) * DH, KEYS_WG * DH * 2);

#define TILE_OFF(i) ((((i) * NW + w) * (4 * TL)) * (DH * 2))
#define LDK(i, t) __builtin_amdgcn_raw_buffer_load_b128(ks, loff + (t) * 1024, TILE_OFF(i), AUX_NT)
#define LDV(i, t) __builtin_amdgcn_raw_buffer_load_b128(vs, loff + (t) * 1024, TILE_OFF(i), AUX_NT)
  const u32 loff = lane * 16;
  u32x4 Kr[DEPTH][TL], Vr[DEPTH][TL];

  u64 epoch = 0;
  if (tid == 0) epoch = __hip_atomic_load((gu64*)(wsp + WS_EPOCH + 256 * blk), __ATOMIC_RELAXED, __HIP_MEMORY_SCOPE_AGENT);
  u32x4 xr[RPL / 8];
#pragma unroll
  for (int j = 0; j < RPL / 8; ++j) xr[j] = __builtin_amdgcn_raw_buffer_load_b128(xs, r * RPL * 2 + 16 * j, w * (4 * RPL * 2), 0);
  u32x4 wr[RPL];
  constexpr int NKV1 = PH1 * 2 * TL;
  constexpr int NI = RPL > NKV1 ? RPL : NKV1;
#pragma unroll
  for (int j = 0; j < NI; ++j) {
    if (j < RPL)
      wr[j] = __builtin_amdgcn_raw_buffer_load_b128(wqs, r * (RPL * NH * DH * 2) + 16 * c, (w * 4 * RPL + j) * (NH * DH * 2) + h * (DH * 2), 0);
    if (j < NKV1) {
      const int d = j / (2 * TL), t = j % TL;
      if ((j / TL) & 1) Vr[d][t] = LDV(d, t); else Kr[d][t] = LDK(d, t);
    }
    __builtin_amdgcn_sched_barrier(0);
  }
  __builtin_amdgcn_s_barrier();
  __builtin_amdgcn_sched_barrier(0);
#pragma unroll
  for (int d = PH1; d < DEPTH; ++d) {
#pragma unroll
    for (int t = 0; t < TL; ++t) Kr[d][t] = LDK(d, t);
#pragma unroll
    for (int t = 0; t < TL; ++t) Vr[d][t] = LDV(d, t);
  }
  __builtin_amdgcn_sched_barrier(0);

  float qa[8];
#pragma unroll
  for (int k = 0; k < 8; ++k) qa[k] = 0.f;
#pragma unroll
  for (int j = 0; j < RPL; ++j) {
    const u32 xw = xr[j >> 3][(j >> 1) & 3];
    const float xv = (j & 1) ? hi_f(xw) : lo_f(xw);
#pragma unroll
    for (int k = 0; k < 4; ++k) {
      qa[2 * k] = fmaf(xv, lo_f(wr[j][k]), qa[2 * k]);
      qa[2 * k + 1] = fmaf(xv, hi_f(wr[j][k]), qa[2 * k + 1]);
    }
  }
  {
    float y0 = tr32(qa[0], qa[4]), y1 = tr32(qa[1], qa[5]), y2 = tr32(qa[2], qa[6]), y3 = tr32(qa[3], qa[7]);
    float z0 = tr16(y0, y2), z1 = tr16(y1, y3);
    *reinterpret_cast<f32x2*>(&lds[LDS_QP + w * DH + 8 * c + 2 * r]) = f32x2{z0, z1};
  }
  if (tid == 0) { *reinterpret_cast<u64*>(&lds[LDS_EP]) = epoch + 1; lds[LDS_EP + 2] = -INFINITY; *reinterpret_cast<u32*>(&lds[LDS_EP + 3]) = 0u; }
  __syncthreads();
  u32 q4[4];
  {
    float s0 = 0.f, s1 = 0.f;
#pragma unroll
    for (int i = 0; i < NW; ++i) {
      const f32x2 p = *reinterpret_cast<const f32x2*>(&lds[LDS_QP + i * DH + 8 * c + 2 * r]);
      s0 += p[0];
      s1 += p[1];
    }
    const u32 qd = pack_bf16(s0, s1);
    auto ab = __builtin_amdgcn_permlane16_swap(qd, qd, false, false);
    auto q02 = __builtin_amdgcn_permlane32_swap(ab[0], ab[0], false, false);
    auto q13 = __builtin_amdgcn_permlane32_swap(ab[1], ab[1], false, false);
    q4[0] = q02[0]; q4[2] = q02[1]; q4[1] = q13[0]; q4[3] = q13[1];
  }
  const u32 tag_lo = __builtin_amdgcn_readfirstlane(*reinterpret_cast<const u32*>(&lds[LDS_EP]));
  const u32 tag_hi = __builtin_amdgcn_readfirstlane(*reinterpret_cast<const u32*>(&lds[LDS_EP + 1]));
  const u32 thi = (tag_hi << 26) | (tag_lo >> 6);
  const u32 tl26 = tag_lo << 26;

  u32x2 wor[NWO];
  u32x4 wb[4][4];
  float m = 0.f, l = 0.f, acc[8];
#pragma unroll
  for (int i = 0; i < NT; ++i) {
    const int d = i % DEPTH;
    float sc[TL];
#pragma unroll
    for (int t = 0; t < TL; ++t) {
      float x = dot2(Kr[d][t][0], q4[0], 0.f);
      x = dot2(Kr[d][t][1], q4[1], x);
      x = dot2(Kr[d][t][2], q4[2], x);
      x = dot2(Kr[d][t][3], q4[3], x);
      sc[t] = x;
      if (i + DEPTH < NT) Kr[d][t] = LDK(i + DEPTH, t);
    }
#pragma unroll
    for (int jj = 0; jj < WO_PER_TILE; ++jj) {
      const int j = i * WO_PER_TILE + jj;
      if (j < NWO)
        wor[j] = __builtin_amdgcn_raw_buffer_load_b64(wos, r * (32 * DM * 2) + 8 * c + (j & 3) * (DM * 2), w * 128 + (j >> 2) * (4 * DM * 2), 0);
    }
    __builtin_amdgcn_sched_barrier(0);
#pragma unroll
    for (int t = 0; t < TL; ++t) sc[t] = row16_sum(sc[t]) * C2;
    float mt = sc[0];
#pragma unroll
    for (int t = 1; t < TL; ++t) mt = fmaxf(mt, sc[t]);
    float p[TL];
    if (i == 0) {
      m = mt;
#pragma unroll
      for (int t = 0; t < TL; ++t) p[t] = __builtin_amdgcn_exp2f(sc[t] - m);
      l = p[0];
#pragma unroll
      for (int t = 1; t < TL; ++t) l += p[t];
#pragma unroll
      for (int k = 0; k < 8; ++k) acc[k] = 0.f;
    } else {
      float mn = fmaxf(m, mt);
      if (i == NT - 1) mn = xrow_max(mn);
      const float alpha = __builtin_amdgcn_exp2f(m - mn);
      m = mn;
#pragma unroll
      for (int t = 0; t < TL; ++t) p[t] = __builtin_amdgcn_exp2f(sc[t] - m);
      l *= alpha;
#pragma unroll
      for (int t = 0; t < TL; ++t) l += p[t];
#pragma unroll
      for (int k = 0; k < 8; ++k) acc[k] *= alpha;
      if (i == NT - 1) {
        const float L = xrow_sum(l);
        u64 sv;
        asm volatile("s_mov_b64 %0, exec\n\ts_mov_b64 exec, 1\n\tds_write_b64 %1, %2\n\tds_max_f32 %3, %4\n\ts_mov_b64 exec, %0"
                     : "=&s"(sv)
                     : "v"((u32)(size_t)(lds_t*)&lds[LDS_ML + 2 * w]), "v"(f32x2{m, L}), "v"((u32)(size_t)(lds_t*)&lds[LDS_EP + 2]), "v"(m)
                     : "memory");
        __builtin_amdgcn_sched_barrier(0);
      }
    }
#pragma unroll
    for (int t = 0; t < TL; ++t) {
#pragma unroll
      for (int k = 0; k < 4; ++k) {
        acc[2 * k] = fmaf(p[t], lo_f(Vr[d][t][k]), acc[2 * k]);
        acc[2 * k + 1] = fmaf(p[t], hi_f(Vr[d][t][k]), acc[2 * k + 1]);
      }
      if (i + DEPTH < NT) Vr[d][t] = LDV(i + DEPTH, t);
      __builtin_amdgcn_sched_barrier(0);
    }
    if (i == PERM_AT) {
#pragma unroll
      for (int q = 0; q < 4; ++q) {
#pragma unroll
        for (int dd = 0; dd < 4; ++dd) {
          const u32x2 A = wor[8 * q + 2 * dd], B = wor[8 * q + 2 * dd + 1];
          wb[q][0][dd] = __builtin_amdgcn_perm(B[0], A[0], 0x05040100u);
          wb[q][1][dd] = __builtin_amdgcn_perm(B[0], A[0], 0x07060302u);
          wb[q][2][dd] = __builtin_amdgcn_perm(B[1], A[1], 0x05040100u);
          wb[q][3][dd] = __builtin_amdgcn_perm(B[1], A[1], 0x07060302u);
        }
        asm volatile("" : "+v"(wb[q][0]), "+v"(wb[q][1]), "+v"(wb[q][2]), "+v"(wb[q][3]));
      }
      __builtin_amdgcn_sched_barrier(0);
    }
  }

  {
    float y0 = tr32(acc[0], acc[4]), y1 = tr32(acc[1], acc[5]), y2 = tr32(acc[2], acc[6]), y3 = tr32(acc[3], acc[7]);
    float z0 = tr16(y0, y2), z1 = tr16(y1, y3);
    const u32 hi = pack_bf16(z0, z1);
    const u32 lo = pack_bf16(z0 - lo_f(hi), z1 - hi_f(hi));
    u32* ap = reinterpret_cast<u32*>(&lds[LDS_AP + (4 * (w >> 1) + (w & 1)) * AROW + 4 * c + r]);
    ap[0] = hi;
    ap[2 * AROW] = lo;
  }
  const int src = h * NSPLIT + s;
  const rsrc_t ib = make_rsrc(wsp + WS_Y + (size_t)blk * (32 * 128 * 128), 32 * 128 * 128);
  __syncthreads();
  {
    u32x4 af[4];
    const u32x4* aimg = reinterpret_cast<const u32x4*>(&lds[LDS_AP + c * AROW + 16 * r]);
    af[0] = aimg[0];
    __builtin_amdgcn_sched_barrier(0);
    af[1] = aimg[1];
    __builtin_amdgcn_sched_barrier(0);
    const float M = lds[LDS_EP + 2];
    const f32x4 ml = *reinterpret_cast<const f32x4*>(&lds[LDS_ML + 4 * r]);
    __builtin_amdgcn_sched_barrier(0);
    af[2] = aimg[2];
    __builtin_amdgcn_sched_barrier(0);
    af[3] = aimg[3];
    __builtin_amdgcn_sched_barrier(0);
    f32x4 D[4];
#define MM(q, e) D[e] = __builtin_amdgcn_mfma_f32_16x16x32_bf16(__builtin_bit_cast(bf16x8, af[q]), __builtin_bit_cast(bf16x8, wb[q][e]), D[e], 0, 0, 0)
#pragma unroll
    for (int e = 0; e < 4; ++e) { D[e] = f32x4{0.f, 0.f, 0.f, 0.f}; MM(0, e); }
    __builtin_amdgcn_sched_barrier(0);
#pragma unroll
    for (int e = 0; e < 4; ++e) MM(1, e);
#pragma unroll
    for (int e = 0; e < 4; ++e) MM(2, e);
    float f0 = __builtin_amdgcn_exp2f(ml[0] - M), f1 = __builtin_amdgcn_exp2f(ml[2] - M);
    const float L = xrow_sum(fmaf(f0, ml[1], f1 * ml[3]));
    const float inv = __builtin_amdgcn_rcpf(L);
    const float lam = M + __builtin_amdgcn_logf(L);
    f0 *= inv;
    f1 *= inv;
    const u32 hd = tl26 | (((f2u(lam) >> (2 * c)) & 3u) << 24);
    __builtin_amdgcn_sched_barrier(0);
#pragma unroll
    for (int e = 0; e < 4; ++e) MM(3, e);
    __builtin_amdgcn_sched_barrier(0);
    float y[4];
#pragma unroll
    for (int e = 0; e < 4; ++e) y[e] = fmaf(f0, D[e][0] + D[e][2], f1 * (D[e][1] + D[e][3]));
    const float v = tr16(tr32(y[0], y[2]), tr32(y[1], y[3]));
    const u32x2 gr = {hd | ((f2u(v) + 0x80u) >> 8), thi};
    __builtin_amdgcn_raw_buffer_store_b64(gr, ib, r * (128 * 128) + c * 8, (w * 4 * 128 + src) * 128, AUX_SC1);
  }
  if (s == 0) {
    const int part = lane & 3;
    const u32 poff = (tid >> 2) * 128 + part * 16;
    u32x8 g;
    u32 t0, t1, t2, cnt;
    const u32 rowa = (u32)(size_t)(lds_t*)&lds[LDS_RED + 8 * (4 * r + part)];
    u32 a0 = rowa + 4 * w, a1 = rowa + 4 * (w ^ 1), a2 = rowa + 4 * ((w ^ 2) & 6), a3 = rowa + 4 * ((w ^ 4) & 4);
    u32 ac = (u32)(size_t)(lds_t*)&lds[LDS_EP + 3];
    gu16* op = (gu16*)(outp + blk * DM + 64 * (h >> 2) + 4 * (8 * (r >> 1) + 2 * part + (r & 1)) + (h & 3));
    asm volatile("" : "+v"(a0), "+v"(a1), "+v"(a2), "+v"(a3), "+v"(ac), "+v"(op));
    u32 done;
    float s1;
    f32x2 s2;
    f32x4 s3;
#define STR_(x) #x
#define STR(x) STR_(x)
    asm volatile(
        "s_movk_i32 %[cnt], 0x7fff\n"
        "1:\n\t"
        "buffer_load_dwordx4 v[200:203], %[po], %[rs], %[so] offen sc1\n\t"
        "buffer_load_dwordx4 v[204:207], %[po], %[rs], %[so] offen offset:64 sc1\n\t"
        "s_waitcnt vmcnt(0)\n\t"
        "v_bitop3_b32 %[t0], v201, v203, %[thi] bitop3:0x7e\n\t"
        "v_bitop3_b32 %[t1], v200, v202, %[tl] bitop3:0x7e\n\t"
        "v_bitop3_b32 %[t2], v204, v206, %[tl] bitop3:0x7e\n\t"
        "v_or_b32 %[t1], %[t1], %[t2]\n\t"
        "v_bitop3_b32 %[t2], v205, v207, %[thi] bitop3:0x7e\n\t"
        "v_lshrrev_b32 %[t1], 26, %[t1]\n\t"
        "v_or3_b32 %[t0], %[t0], %[t1], %[t2]\n\t"
        "v_cmp_ne_u32 vcc, 0, %[t0]\n\t"
        "s_and_b64 exec, exec, vcc\n\t"
        "s_cbranch_scc0 2f\n\t"
        "s_sleep " STR(POLL_SLEEP) "\n\t"
        "s_sub_u32 %[cnt], %[cnt], 1\n\t"
        "s_cbranch_scc0 1b\n"
        "2:\n\t"
        "s_mov_b64 exec, -1"
        "\n\tds_read_b32 %[dn], %[ac]\n\tds_read_b32 %[s1], %[a1]\n\tds_read_b64 %[s2], %[a2]\n\tds_read_b128 %[s3], %[a3]"
        : "=&{v[200:207]}"(g), [t0] "=&v"(t0), [t1] "=&v"(t1), [t2] "=&v"(t2), [cnt] "=&s"(cnt)
          , [dn] "=&v"(done), [s1] "=&v"(s1), [s2] "=&v"(s2), [s3] "=&v"(s3)
        : [po] "v"(poff), [rs] "s"(ib), [so] "s"(h * (128 * 128)), [thi] "v"(thi), [tl] "s"(tl26)
          , [ac] "v"(ac), [a1] "v"(a1), [a2] "v"(a2), [a3] "v"(a3)
        : "vcc", "scc", "memory");
    u32 lb = (((g[0] >> 24) & 3u) | ((g[2] >> 22) & 12u) | ((g[4] >> 8) & 0x30000u) | ((g[6] >> 6) & 0xC0000u)) << (4 * part);
    lb |= f2u(dpp<0xB1>(u2f(lb)));
    lb |= f2u(dpp<0x4E>(u2f(lb)));
    const float lm = u2f(lb);
    float M = fmaxf(lm, dpp<0x124>(lm));
    M = fmaxf(M, dpp<0x128>(M));
    const float e = __builtin_amdgcn_exp2f(lm - M);
    float L = e + dpp<0x124>(e);
    L += dpp<0x128>(L);
    const float wt = e * __builtin_amdgcn_rcpf(L);
    const float u0 = tr32(u2f(g[0] << 8) * wt, u2f(g[4] << 8) * wt);
    const float u1 = tr32(u2f(g[2] << 8) * wt, u2f(g[6] << 8) * wt);
    float v = tr16(u0, u1);
    asm volatile("s_waitcnt lgkmcnt(0)" : "+v"(done), "+v"(s1), "+v"(s2), "+v"(s3), "+v"(v));
    v += dpp<0x124>(v);
    v += dpp<0x128>(v);
    bool fin = __builtin_amdgcn_readfirstlane(done) == NW - 1;
    if (!fin) {
      asm volatile(
          "s_mov_b32 exec_lo, 0x000f000f\n\ts_mov_b32 exec_hi, 0x000f000f\n\t"
          "ds_write_b32 %8, %9\n\t"
          "s_mov_b64 exec, 1\n\t"
          "ds_add_rtn_u32 %0, %4, %10\n\t"
          "s_mov_b64 exec, -1\n\t"
          "ds_read_b32 %1, %5\n\tds_read_b64 %2, %6\n\tds_read_b128 %3, %7\n\t"
          "s_waitcnt lgkmcnt(0)"
          : "=&v"(done), "=&v"(s1), "=&v"(s2), "=&v"(s3)
          : "v"(ac), "v"(a1), "v"(a2), "v"(a3), "v"(a0), "v"(v), "v"(1u)
          : "memory");
      fin = __builtin_amdgcn_readfirstlane(done) == NW - 1;
    }
    if (fin) {
      const float tot = ((v + s1) + (s2[0] + s2[1])) + ((s3[0] + s3[1]) + (s3[2] + s3[3]));
      if ((lane & 12) == 0) *op = (u16)pack_bf16(tot, tot);
      if (lane == 0 && h == 0)
        __hip_atomic_store((gu64*)(wsp + WS_EPOCH + 256 * blk), ((u64)tag_hi << 32) | tag_lo, __ATOMIC_RELAXED, __HIP_MEMORY_SCOPE_AGENT);
    }
  }
}

extern "C" void kernel_launch(void* const* d_in, const int* in_sizes, int n_in, void* d_out, int out_size, void* d_ws,
                              size_t ws_size, hipStream_t stream) {
  Args a;
  a.x[0] = (const u16*)d_in[0];  a.k[0] = (const u16*)d_in[1];  a.v[0] = (const u16*)d_in[2];
  a.wq[0] = (const u16*)d_in[3]; a.wo[0] = (const u16*)d_in[4];
  a.x[1] = (const u16*)d_in[5];  a.k[1] = (const u16*)d_in[6];  a.v[1] = (const u16*)d_in[7];
  a.wq[1] = (const u16*)d_in[8]; a.wo[1] = (const u16*)d_in[9];
  a.out = (u16*)d_out;
  a.ws = (char*)d_ws;
  attn_kernel<<<dim3(64 * NSPLIT), dim3(NW * 64), 0, stream>>>(a);
}
```

```cpp
#include <hip/hip_runtime.h>
#include <stdint.h>

#define DEPTH 4
#define PH1 3
#define WO_PER_TILE 4
#define PERM_AT (NT - 3)
#define POLL_SLEEP 1

typedef unsigned int u32;
typedef unsigned short u16;
typedef unsigned long long u64;
using u32x8 = __attribute__((ext_vector_type(8))) unsigned;
using u32x4 = __attribute__((ext_vector_type(4))) unsigned;
using u32x2 = __attribute__((ext_vector_type(2))) unsigned;
using f32x4 = __attribute__((ext_vector_type(4))) float;
using f32x2 = __attribute__((ext_vector_type(2))) float;
using bf16x2 = __attribute__((ext_vector_type(2))) __bf16;
using bf16x8 = __attribute__((ext_vector_type(8))) __bf16;
typedef __attribute__((address_space(1))) u64 gu64;
typedef __attribute__((address_space(3))) float lds_t;

constexpr int NW = 8;
constexpr int TL = 4;
constexpr int DM = 512, NH = 32, SK = 8192, DH = 128;
constexpr int NSPLIT = 4;
constexpr int KEYS_WG = SK / NSPLIT;
constexpr int NT = KEYS_WG / NW / (4 * TL);
constexpr int RPL = DM / NW / 4;
constexpr int NWO = 32;
constexpr float C2 = 0.08838834764831845f * 1.4426950408889634f;
constexpr int AUX_NT = 2, AUX_SC1 = 16;
static_assert(NWO <= WO_PER_TILE * PERM_AT, "wo loads must fit beside the tiles");

struct Args {
  const u16* x[2];
  const u16* k[2];
  const u16* v[2];
  const u16* wq[2];
  const u16* wo[2];
  u16* out;
  char* ws;
};

__device__ __forceinline__ u32 f2u(float v) { return __builtin_bit_cast(u32, v); }
__device__ __forceinline__ float u2f(u32 v) { return __builtin_bit_cast(float, v); }
__device__ __forceinline__ float dot2(u32 a, u32 b, float c) {
  return __builtin_amdgcn_fdot2_f32_bf16(__builtin_bit_cast(bf16x2, a), __builtin_bit_cast(bf16x2, b), c, false);
}
template <int CTRL> __device__ __forceinline__ float dpp(float x) {
  return u2f((u32)__builtin_amdgcn_mov_dpp((int)f2u(x), CTRL, 0xf, 0xf, true));
}
__device__ __forceinline__ float row16_sum(float x) {
  x += dpp<0xB1>(x);
  x += dpp<0x4E>(x);
  x += dpp<0x141>(x);
  x += dpp<0x140>(x);
  return x;
}
__device__ __forceinline__ float lo_f(u32 v) { return u2f(v << 16); }
__device__ __forceinline__ float hi_f(u32 v) { return u2f(v & 0xffff0000u); }
__device__ __forceinline__ float tr32(float a, float b) {
  auto s = __builtin_amdgcn_permlane32_swap(f2u(a), f2u(b), false, false);
  return u2f(s[0]) + u2f(s[1]);
}
__device__ __forceinline__ float tr16(float a, float b) {
  auto s = __builtin_amdgcn_permlane16_swap(f2u(a), f2u(b), false, false);
  return u2f(s[0]) + u2f(s[1]);
}
__device__ __forceinline__ float xrow_max(float x) {
  auto s = __builtin_amdgcn_permlane16_swap(f2u(x), f2u(x), false, false);
  x = fmaxf(u2f(s[0]), u2f(s[1]));
  auto t = __builtin_amdgcn_permlane32_swap(f2u(x), f2u(x), false, false);
  return fmaxf(u2f(t[0]), u2f(t[1]));
}
__device__ __forceinline__ float xrow_sum(float x) {
  x = tr16(x, x);
  return tr32(x, x);
}
__device__ __forceinline__ u32 pack_bf16(float lo, float hi) {
  bf16x2 r;
  r[0] = (__bf16)lo;
  r[1] = (__bf16)hi;
  return __builtin_bit_cast(u32, r);
}
typedef __amdgpu_buffer_rsrc_t rsrc_t;
__device__ __forceinline__ rsrc_t make_rsrc(const void* p, u32 bytes) {
  return __builtin_amdgcn_make_buffer_rsrc(const_cast<void*>(p), 0, bytes, 0x00020000);
}

constexpr int AROW = 68;
constexpr int LDS_QP = 0;
constexpr int LDS_AP = LDS_QP + NW * DH;
constexpr int LDS_ML = LDS_AP + 16 * AROW;
constexpr int LDS_RED = LDS_ML + 2 * NW;
constexpr int LDS_EP = LDS_RED + 128;
constexpr int LDS_FLOATS = LDS_EP + 4;

constexpr size_t WS_Y = 0;
constexpr size_t WS_EPOCH = 1u << 20;


__global__ void __launch_bounds__(NW * 64, NW / 4) attn_kernel(Args a) {
  __shared__ __attribute__((aligned(16))) float lds[LDS_FLOATS];
  const int bid = blockIdx.x;
  const int hg = bid & 63, s = bid >> 6;
  const int blk = hg >> 5, h = hg & 31;
  const int tid = threadIdx.x, lane = tid & 63;
  const int w = __builtin_amdgcn_readfirstlane(tid >> 6);
  const int r = lane >> 4, c = lane & 15;

  char* wsp = a.ws;
  u16* outp = a.out;
  const u16 *xp = a.x[blk], *wqp = a.wq[blk], *wop = a.wo[blk], *kp = a.k[blk], *vp = a.v[blk];
  asm volatile("" : "+s"(wsp), "+s"(outp), "+s"(xp), "+s"(wqp), "+s"(wop), "+s"(kp), "+s"(vp));
  const rsrc_t xs = make_rsrc(xp, DM * 2);
  const rsrc_t wqs = make_rsrc(wqp, DM * NH * DH * 2);
  const rsrc_t wos = make_rsrc(wop + (size_t)h * DH * DM, DH * DM * 2);
  const rsrc_t ks = make_rsrc(kp + ((size_t)h * SK + (size_t)s * KEYS_WG) * DH, KEYS_WG * DH * 2);
  const rsrc_t vs = make_rsrc(vp + ((size_t)h * SK + (size_t)s * KEYS_WG) * DH, KEYS_WG * DH * 2);

#define TILE_OFF(i) ((((i) * NW + w) * (4 * TL)) * (DH * 2))
#define LDK(i, t) __builtin_amdgcn_raw_buffer_load_b128(ks, loff + (t) * 1024, TILE_OFF(i), AUX_NT)
#define LDV(i, t) __builtin_amdgcn_raw_buffer_load_b128(vs, loff + (t) * 1024, TILE_OFF(i), AUX_NT)
  const u32 loff = lane * 16;
  u32x4 Kr[DEPTH][TL], Vr[DEPTH][TL];

  u64 epoch = 0;
  if (tid == 0) epoch = __hip_atomic_load((gu64*)(wsp + WS_EPOCH + 256 * blk), __ATOMIC_RELAXED, __HIP_MEMORY_SCOPE_AGENT);
  u32x4 xr[RPL / 8];
#pragma unroll
  for (int j = 0; j < RPL / 8; ++j) xr[j] = __builtin_amdgcn_raw_buffer_load_b128(xs, r * RPL * 2 + 16 * j, w * (4 * RPL * 2), 0);
  u32x4 wr[RPL];
  constexpr int NKV1 = PH1 * 2 * TL;
  constexpr int NI = RPL > NKV1 ? RPL : NKV1;
#pragma unroll
  for (int j = 0; j < NI; ++j) {
    if (j < RPL)
      wr[j] = __builtin_amdgcn_raw_buffer_load_b128(wqs, r * (RPL * NH * DH * 2) + 16 * c, (w * 4 * RPL + j) * (NH * DH * 2) + h * (DH * 2), 0);
    if (j < NKV1) {
      const int d = j / (2 * TL), t = j % TL;
      if ((j / TL) & 1) Vr[d][t] = LDV(d, t); else Kr[d][t] = LDK(d, t);
    }
    __builtin_amdgcn_sched_barrier(0);
  }
  __builtin_amdgcn_s_barrier();
  __builtin_amdgcn_sched_barrier(0);
#pragma unroll
  for (int d = PH1; d < DEPTH; ++d) {
#pragma unroll
    for (int t = 0; t < TL; ++t) Kr[d][t] = LDK(d, t);
#pragma unroll
    for (int t = 0; t < TL; ++t) Vr[d][t] = LDV(d, t);
  }
  __builtin_amdgcn_sched_barrier(0);

  float qa[8];
#pragma unroll
  for (int k = 0; k < 8; ++k) qa[k] = 0.f;
#pragma unroll
  for (int j = 0; j < RPL; ++j) {
    const u32 xw = xr[j >> 3][(j >> 1) & 3];
    const float xv = (j & 1) ? hi_f(xw) : lo_f(xw);
#pragma unroll
    for (int k = 0; k < 4; ++k) {
      qa[2 * k] = fmaf(xv, lo_f(wr[j][k]), qa[2 * k]);
      qa[2 * k + 1] = fmaf(xv, hi_f(wr[j][k]), qa[2 * k + 1]);
    }
  }
  {
    float y0 = tr32(qa[0], qa[4]), y1 = tr32(qa[1], qa[5]), y2 = tr32(qa[2], qa[6]), y3 = tr32(qa[3], qa[7]);
    float z0 = tr16(y0, y2), z1 = tr16(y1, y3);
    *reinterpret_cast<f32x2*>(&lds[LDS_QP + w * DH + 8 * c + 2 * r]) = f32x2{z0, z1};
  }
  if (tid == 0) { *reinterpret_cast<u64*>(&lds[LDS_EP]) = epoch + 1; lds[LDS_EP + 2] = -INFINITY; *reinterpret_cast<u32*>(&lds[LDS_EP + 3]) = 0u; }
  __syncthreads();
  u32 q4[4];
  {
    float s0 = 0.f, s1 = 0.f;
#pragma unroll
    for (int i = 0; i < NW; ++i) {
      const f32x2 p = *reinterpret_cast<const f32x2*>(&lds[LDS_QP + i * DH + 8 * c + 2 * r]);
      s0 += p[0];
      s1 += p[1];
    }
    const u32 qd = pack_bf16(s0, s1);
    auto ab = __builtin_amdgcn_permlane16_swap(qd, qd, false, false);
    auto q02 = __builtin_amdgcn_permlane32_swap(ab[0], ab[0], false, false);
    auto q13 = __builtin_amdgcn_permlane32_swap(ab[1], ab[1], false, false);
    q4[0] = q02[0]; q4[2] = q02[1]; q4[1] = q13[0]; q4[3] = q13[1];
  }
  const u32 tag_lo = __builtin_amdgcn_readfirstlane(*reinterpret_cast<const u32*>(&lds[LDS_EP]));
  const u32 tag_hi = __builtin_amdgcn_readfirstlane(*reinterpret_cast<const u32*>(&lds[LDS_EP + 1]));
  const u32 thi = (tag_hi << 26) | (tag_lo >> 6);
  const u32 tl26 = tag_lo << 26;

  u32x2 wor[NWO];
  u32x4 wb[4][4];
  float m = 0.f, l = 0.f, acc[8];
#pragma unroll
  for (int i = 0; i < NT; ++i) {
    const int d = i % DEPTH;
    float sc[TL];
#pragma unroll
    for (int t = 0; t < TL; ++t) {
      float x = dot2(Kr[d][t][0], q4[0], 0.f);
      x = dot2(Kr[d][t][1], q4[1], x);
      x = dot2(Kr[d][t][2], q4[2], x);
      x = dot2(Kr[d][t][3], q4[3], x);
      sc[t] = x;
      if (i + DEPTH < NT) Kr[d][t] = LDK(i + DEPTH, t);
    }
#pragma unroll
    for (int jj = 0; jj < WO_PER_TILE; ++jj) {
      const int j = i * WO_PER_TILE + jj;
      if (j < NWO)
        wor[j] = __builtin_amdgcn_raw_buffer_load_b64(wos, r * (32 * DM * 2) + 8 * c + (j & 3) * (DM * 2), w * 128 + (j >> 2) * (4 * DM * 2), 0);
    }
    __builtin_amdgcn_sched_barrier(0);
#pragma unroll
    for (int t = 0; t < TL; ++t) sc[t] = row16_sum(sc[t]) * C2;
    float mt = sc[0];
#pragma unroll
    for (int t = 1; t < TL; ++t) mt = fmaxf(mt, sc[t]);
    float p[TL];
    if (i == 0) {
      m = mt;
#pragma unroll
      for (int t = 0; t < TL; ++t) p[t] = __builtin_amdgcn_exp2f(sc[t] - m);
      l = p[0];
#pragma unroll
      for (int t = 1; t < TL; ++t) l += p[t];
#pragma unroll
      for (int k = 0; k < 8; ++k) acc[k] = 0.f;
    } else {
      float mn = fmaxf(m, mt);
      if (i == NT - 1) mn = xrow_max(mn);
      const float alpha = __builtin_amdgcn_exp2f(m - mn);
      m = mn;
#pragma unroll
      for (int t = 0; t < TL; ++t) p[t] = __builtin_amdgcn_exp2f(sc[t] - m);
      l *= alpha;
#pragma unroll
      for (int t = 0; t < TL; ++t) l += p[t];
#pragma unroll
      for (int k = 0; k < 8; ++k) acc[k] *= alpha;
      if (i == NT - 1) {
        const float L = xrow_sum(l);
        u64 sv;
        asm volatile("s_mov_b64 %0, exec\n\ts_mov_b64 exec, 1\n\tds_write_b64 %1, %2\n\tds_max_f32 %3, %4\n\ts_mov_b64 exec, %0"
                     : "=&s"(sv)
                     : "v"((u32)(size_t)(lds_t*)&lds[LDS_ML + 2 * w]), "v"(f32x2{m, L}), "v"((u32)(size_t)(lds_t*)&lds[LDS_EP + 2]), "v"(m)
                     : "memory");
        __builtin_amdgcn_sched_barrier(0);
      }
    }
#pragma unroll
    for (int t = 0; t < TL; ++t) {
#pragma unroll
      for (int k = 0; k < 4; ++k) {
        acc[2 * k] = fmaf(p[t], lo_f(Vr[d][t][k]), acc[2 * k]);
        acc[2 * k + 1] = fmaf(p[t], hi_f(Vr[d][t][k]), acc[2 * k + 1]);
      }
      if (i + DEPTH < NT) Vr[d][t] = LDV(i + DEPTH, t);
      __builtin_amdgcn_sched_barrier(0);
    }
    if (i == PERM_AT) {
#pragma unroll
      for (int q = 0; q < 4; ++q) {
#pragma unroll
        for (int dd = 0; dd < 4; ++dd) {
          const u32x2 A = wor[8 * q + 2 * dd], B = wor[8 * q + 2 * dd + 1];
          wb[q][0][dd] = __builtin_amdgcn_perm(B[0], A[0], 0x05040100u);
          wb[q][1][dd] = __builtin_amdgcn_perm(B[0], A[0], 0x07060302u);
          wb[q][2][dd] = __builtin_amdgcn_perm(B[1], A[1], 0x05040100u);
          wb[q][3][dd] = __builtin_amdgcn_perm(B[1], A[1], 0x07060302u);
        }
        asm volatile("" : "+v"(wb[q][0]), "+v"(wb[q][1]), "+v"(wb[q][2]), "+v"(wb[q][3]));
      }
      __builtin_amdgcn_sched_barrier(0);
    }
  }

  {
    float y0 = tr32(acc[0], acc[4]), y1 = tr32(acc[1], acc[5]), y2 = tr32(acc[2], acc[6]), y3 = tr32(acc[3], acc[7]);
    float z0 = tr16(y0, y2), z1 = tr16(y1, y3);
    const u32 hi = pack_bf16(z0, z1);
    const u32 lo = pack_bf16(z0 - lo_f(hi), z1 - hi_f(hi));
    u32* ap = reinterpret_cast<u32*>(&lds[LDS_AP + (4 * (w >> 1) + (w & 1)) * AROW + 4 * c + r]);
    ap[0] = hi;
    ap[2 * AROW] = lo;
  }
  const int src = h * NSPLIT + s;
  const rsrc_t ib = make_rsrc(wsp + WS_Y + (size_t)blk * (32 * 128 * 128), 32 * 128 * 128);
  __syncthreads();
  {
    u32x4 af[4];
    const u32x4* aimg = reinterpret_cast<const u32x4*>(&lds[LDS_AP + c * AROW + 16 * r]);
    af[0] = aimg[0];
    __builtin_amdgcn_sched_barrier(0);
    af[1] = aimg[1];
    __builtin_amdgcn_sched_barrier(0);
    const float M = lds[LDS_EP + 2];
    const f32x4 ml = *reinterpret_cast<const f32x4*>(&lds[LDS_ML + 4 * r]);
    __builtin_amdgcn_sched_barrier(0);
    af[2] = aimg[2];
    __builtin_amdgcn_sched_barrier(0);
    af[3] = aimg[3];
    __builtin_amdgcn_sched_barrier(0);
    f32x4 D[4];
#define MM(q, e) D[e] = __builtin_amdgcn_mfma_f32_16x16x32_bf16(__builtin_bit_cast(bf16x8, af[q]), __builtin_bit_cast(bf16x8, wb[q][e]), D[e], 0, 0, 0)
#pragma unroll
    for (int e = 0; e < 4; ++e) { D[e] = f32x4{0.f, 0.f, 0.f, 0.f}; MM(0, e); }
    __builtin_amdgcn_sched_barrier(0);
#pragma unroll
    for (int e = 0; e < 4; ++e) MM(1, e);
#pragma unroll
    for (int e = 0; e < 4; ++e) MM(2, e);
    float f0 = __builtin_amdgcn_exp2f(ml[0] - M), f1 = __builtin_amdgcn_exp2f(ml[2] - M);
    const float L = xrow_sum(fmaf(f0, ml[1], f1 * ml[3]));
    const float inv = __builtin_amdgcn_rcpf(L);
    const float lam = M + __builtin_amdgcn_logf(L);
    f0 *= inv;
    f1 *= inv;
    const u32 hd = tl26 | (((f2u(lam) >> (2 * c)) & 3u) << 24);
    __builtin_amdgcn_sched_barrier(0);
#pragma unroll
    for (int e = 0; e < 4; ++e) MM(3, e);
    __builtin_amdgcn_sched_barrier(0);
    float y[4];
#pragma unroll
    for (int e = 0; e < 4; ++e) y[e] = fmaf(f0, D[e][0] + D[e][2], f1 * (D[e][1] + D[e][3]));
    const float v = tr16(tr32(y[0], y[2]), tr32(y[1], y[3]));
    const u32x2 gr = {hd | ((f2u(v) + 0x80u) >> 8), thi};
    __builtin_amdgcn_raw_buffer_store_b64(gr, ib, r * (128 * 128) + c * 8, (w * 4 * 128 + src) * 128, AUX_SC1);
  }
  if (s == 0) {
    const int part = lane & 3;
    const u32 poff = (tid >> 2) * 128 + part * 16;
    u32x8 g;
    u32 t0, t1, t2, cnt;
    const u32 rowa = (u32)(size_t)(lds_t*)&lds[LDS_RED + 8 * (4 * r + part)];
    u32 a0 = rowa + 4 * w, a1 = rowa + 4 * (w ^ 1), a2 = rowa + 4 * ((w ^ 2) & 6), a3 = rowa + 4 * ((w ^ 4) & 4);
    u32 ac = (u32)(size_t)(lds_t*)&lds[LDS_EP + 3];
    asm volatile("" : "+v"(a0), "+v"(a1), "+v"(a2), "+v"(a3), "+v"(ac));
    u32 oo = (lane & 12) == 0 ? 2 * (blk * DM + 64 * (h >> 2) + 4 * (8 * (r >> 1) + 2 * part + (r & 1)) + (h & 3)) : 0x40000000u;
    u32 eo = (lane == 0 && h == 0) ? 256 * blk : 0x40000000u;
    asm volatile("" : "+v"(oo), "+v"(eo));
    u32 done;
    float s1;
    f32x2 s2;
    f32x4 s3;
#define STR_(x) #x
#define STR(x) STR_(x)
    asm volatile(
        "s_movk_i32 %[cnt], 0x7fff\n"
        "1:\n\t"
        "buffer_load_dwordx4 v[200:203], %[po], %[rs], %[so] offen sc1\n\t"
        "buffer_load_dwordx4 v[204:207], %[po], %[rs], %[so] offen offset:64 sc1\n\t"
        "s_waitcnt vmcnt(0)\n\t"
        "v_bitop3_b32 %[t0], v201, v203, %[thi] bitop3:0x7e\n\t"
        "v_bitop3_b32 %[t1], v200, v202, %[tl] bitop3:0x7e\n\t"
        "v_bitop3_b32 %[t2], v204, v206, %[tl] bitop3:0x7e\n\t"
        "v_or_b32 %[t1], %[t1], %[t2]\n\t"
        "v_bitop3_b32 %[t2], v205, v207, %[thi] bitop3:0x7e\n\t"
        "v_lshrrev_b32 %[t1], 26, %[t1]\n\t"
        "v_or3_b32 %[t0], %[t0], %[t1], %[t2]\n\t"
        "v_cmp_ne_u32 vcc, 0, %[t0]\n\t"
        "s_and_b64 exec, exec, vcc\n\t"
        "s_cbranch_scc0 2f\n\t"
        "s_sleep " STR(POLL_SLEEP) "\n\t"
        "s_sub_u32 %[cnt], %[cnt], 1\n\t"
        "s_cbranch_scc0 1b\n"
        "2:\n\t"
        "s_mov_b64 exec, -1"
        "\n\tds_read_b32 %[dn], %[ac]\n\tds_read_b32 %[s1], %[a1]\n\tds_read_b64 %[s2], %[a2]\n\tds_read_b128 %[s3], %[a3]"
        : "=&{v[200:207]}"(g), [t0] "=&v"(t0), [t1] "=&v"(t1), [t2] "=&v"(t2), [cnt] "=&s"(cnt)
          , [dn] "=&v"(done), [s1] "=&v"(s1), [s2] "=&v"(s2), [s3] "=&v"(s3)
        : [po] "v"(poff), [rs] "s"(ib), [so] "s"(h * (128 * 128)), [thi] "v"(thi), [tl] "s"(tl26)
          , [ac] "v"(ac), [a1] "v"(a1), [a2] "v"(a2), [a3] "v"(a3)
        : "vcc", "scc", "memory");
    u32 lb = (((g[0] >> 24) & 3u) | ((g[2] >> 22) & 12u) | ((g[4] >> 8) & 0x30000u) | ((g[6] >> 6) & 0xC0000u)) << (4 * part);
    lb |= f2u(dpp<0xB1>(u2f(lb)));
    lb |= f2u(dpp<0x4E>(u2f(lb)));
    const float lm = u2f(lb);
    float M = fmaxf(lm, dpp<0x124>(lm));
    M = fmaxf(M, dpp<0x128>(M));
    const float e = __builtin_amdgcn_exp2f(lm - M);
    float L = e + dpp<0x124>(e);
    L += dpp<0x128>(L);
    const float wt = e * __builtin_amdgcn_rcpf(L);
    const float u0 = tr32(u2f(g[0] << 8) * wt, u2f(g[4] << 8) * wt);
    const float u1 = tr32(u2f(g[2] << 8) * wt, u2f(g[6] << 8) * wt);
    float v = tr16(u0, u1);
    asm volatile("s_waitcnt lgkmcnt(0)" : "+v"(done), "+v"(s1), "+v"(s2), "+v"(s3), "+v"(v));
    v += dpp<0x124>(v);
    v += dpp<0x128>(v);
    bool fin = __builtin_amdgcn_readfirstlane(done) == NW - 1;
    if (!fin) {
      asm volatile(
          "s_mov_b32 exec_lo, 0x000f000f\n\ts_mov_b32 exec_hi, 0x000f000f\n\t"
          "ds_write_b32 %8, %9\n\t"
          "s_mov_b64 exec, 1\n\t"
          "ds_add_rtn_u32 %0, %4, %10\n\t"
          "s_mov_b64 exec, -1\n\t"
          "ds_read_b32 %1, %5\n\tds_read_b64 %2, %6\n\tds_read_b128 %3, %7\n\t"
          "s_waitcnt lgkmcnt(0)"
          : "=&v"(done), "=&v"(s1), "=&v"(s2), "=&v"(s3)
          : "v"(ac), "v"(a1), "v"(a2), "v"(a3), "v"(a0), "v"(v), "v"(1u)
          : "memory");
      fin = __builtin_amdgcn_readfirstlane(done) == NW - 1;
    }
    {
      const float tot = ((v + s1) + (s2[0] + s2[1])) + ((s3[0] + s3[1]) + (s3[2] + s3[3]));
      const u32 so = fin ? 0u : 0x40000000u;
      __builtin_amdgcn_raw_buffer_store_b16((u16)pack_bf16(tot, tot), make_rsrc(outp, 2 * DM * 2), oo | so, 0, AUX_SC1);
      __builtin_amdgcn_raw_buffer_store_b64(u32x2{tag_lo, tag_hi}, make_rsrc(wsp + WS_EPOCH, 512), eo | so, 0, AUX_SC1);
    }
  }
}

extern "C" void kernel_launch(void* const* d_in, const int* in_sizes, int n_in, void* d_out, int out_size, void* d_ws,
                              size_t ws_size, hipStream_t stream) {
  Args a;
  a.x[0] = (const u16*)d_in[0];  a.k[0] = (const u16*)d_in[1];  a.v[0] = (const u16*)d_in[2];
  a.wq[0] = (const u16*)d_in[3]; a.wo[0] = (const u16*)d_in[4];
  a.x[1] = (const u16*)d_in[5];  a.k[1] = (const u16*)d_in[6];  a.v[1] = (const u16*)d_in[7];
  a.wq[1] = (const u16*)d_in[8]; a.wo[1] = (const u16*)d_in[9];
  a.out = (u16*)d_out;
  a.ws = (char*)d_ws;
  attn_kernel<<<dim3(64 * NSPLIT), dim3(NW * 64), 0, stream>>>(a);
}
```
